```python
import torch

def _softmax(x, axis=-1):
    return torch.softmax(x, dim=axis)
def _gelu(x):
    return torch.nn.functional.gelu(x, approximate="tanh")
def _silu(x):
    return torch.nn.functional.silu(x)
def _sigmoid(x):
    return torch.sigmoid(x)
def _layernorm(x, axis=-1, eps=1e-5):
    m = x.mean(dim=axis, keepdim=True)
    v = x.var(dim=axis, keepdim=True, unbiased=False)
    return (x - m) / torch.sqrt(v + eps)
def _scatter_add(tgt, idx, src, axis=0):
    return tgt.clone().index_add_(axis, idx, src)
def _segment_sum(src, seg, n):
    out = torch.zeros((n,) + src.shape[1:], dtype=src.dtype)
    return out.index_add_(0, seg, src)
def _pad(x, pw):
    flat = tuple(v for pair in reversed(pw) for v in pair)
    return torch.nn.functional.pad(x, flat)

kernel_name = 'recurrent_scan__sliding_local_dilated__glu_gated_swiglu__prompt'

def setup_inputs():

    _w = (lambda a: (lambda t: t.to(torch.bfloat16) if t.is_floating_point() else t)(torch.as_tensor(a)))
    def _A_setup():
        B, S, D = 1, 2048, 1024
        return {
            "x": _w((lambda *s: torch.randn(*s, dtype=torch.bfloat16) * 0.05)(B, S, D)),
            "wqkv": _w((lambda *s: torch.randn(*s, dtype=torch.bfloat16) * 0.2)(D, 3 * D)),
            "wo": _w((lambda *s: torch.randn(*s, dtype=torch.bfloat16) * 0.2)(D, D)),
        }
    _A_in = _A_setup()

    def _B_setup():
        B, S, D, F = 1, 2048, 1024, 3584
        return {
            "x": _w((lambda *s: torch.randn(*s, dtype=torch.bfloat16) * 0.05)(B, S, D)),
            "w1": _w((lambda *s: torch.randn(*s, dtype=torch.bfloat16) * 0.1)(D, F) * (2.0 / D) ** 0.5),
            "w3": _w((lambda *s: torch.randn(*s, dtype=torch.bfloat16) * 0.1)(D, F) * (2.0 / D) ** 0.5),
            "w2": _w((lambda *s: torch.randn(*s, dtype=torch.bfloat16) * 0.1)(F, D) * (2.0 / F) ** 0.5),
        }
    _B_in = _B_setup()

    return {
        "A_x": _A_in["x"],
        "A_wqkv": _A_in["wqkv"],
        "A_wo": _A_in["wo"],
        "B_x": _B_in["x"],
        "B_w1": _B_in["w1"],
        "B_w3": _B_in["w3"],
        "B_w2": _B_in["w2"],
    }

def reference(A_x, A_wqkv, A_wo, B_x, B_w1, B_w3, B_w2):
    def _A_ref(x, wqkv, wo):
        B, S, D = x.shape
        H = 32; Dh = D // H
        W, dil = 8, 2
        qkv = (lambda x, *d: x.permute(*d))((x @ wqkv).reshape(B, S, 3, H, Dh), 2, 0, 3, 1, 4)
        q, k, v = qkv[0], qkv[1], qkv[2]
        scale = Dh ** -0.5
        pos = torch.arange(S)
        off = torch.arange(W) * dil
        idx = pos[:, None] - off[None, :]
        mask = idx >= 0
        idx_c = torch.where(mask, idx, 0).reshape(-1)
        kg = k[:, :, idx_c, :].reshape(B, H, S, W, Dh)
        vg = v[:, :, idx_c, :].reshape(B, H, S, W, Dh)
        logits = torch.einsum("bhsd,bhswd->bhsw", q, kg) * scale
        logits = torch.where(mask[None, None, :, :], logits, -1e9)
        attn = _softmax(logits, axis=-1)
        o = torch.einsum("bhsw,bhswd->bhsd", attn, vg)
        return (lambda x, *d: x.permute(*d))(o, 0, 2, 1, 3).reshape(B, S, D) @ wo

    def _B_ref(x, w1, w3, w2):
        gate = torch.einsum("bsd,df->bsf", x, w1)
        up = torch.einsum("bsd,df->bsf", x, w3)
        h = _silu(gate) * up
        out = torch.einsum("bsf,fd->bsd", h, w2)
        return out

    x = A_x
    xn = _layernorm(x, axis=-1)
    s = _A_ref(xn, A_wqkv, A_wo)
    g = _silu(_B_ref(xn, B_w1, B_w3, B_w2))
    return x + s * g
```

```cpp
#include <hip/hip_runtime.h>
#include <hip/hip_bf16.h>
#include <stdint.h>

typedef unsigned short u16;
typedef short bf16x8 __attribute__((ext_vector_type(8)));
typedef short s16x4 __attribute__((ext_vector_type(4)));
typedef float f32x4 __attribute__((ext_vector_type(4)));
typedef unsigned u32x2 __attribute__((ext_vector_type(2)));
typedef unsigned u32x4 __attribute__((ext_vector_type(4)));

#define LDS_AS __attribute__((address_space(3)))
#define WAIT_V0() asm volatile("s_waitcnt vmcnt(0)" ::: "memory")

constexpr int S_ = 2048, D_ = 1024, F_ = 3584, H_ = 32, DH_ = 32, NQKV_ = 3072;

__device__ __forceinline__ float bf2f(u16 b) { return __uint_as_float(((uint32_t)b) << 16); }
__device__ __forceinline__ u16 f2bf(float f) {
  __bf16 b = (__bf16)f;
  return __builtin_bit_cast(u16, b);
}
__device__ __forceinline__ float rbf(float f) { return bf2f(f2bf(f)); }
__device__ __forceinline__ float silu_f(float v) { return v / (1.0f + __expf(-v)); }

__device__ __forceinline__ s16x4 tr_read(const char* lds_ptr) {
  return __builtin_amdgcn_ds_read_tr16_b64_v4i16((LDS_AS s16x4*)(lds_ptr));
}

__device__ __forceinline__ void glds16(const void* gsrc, unsigned lds_dst) {
  unsigned keep;
  asm volatile("s_mov_b32 %0, m0\n\ts_mov_b32 m0, %2\n\ts_nop 0\n\tglobal_load_lds_dwordx4 %1, off\n\ts_mov_b32 m0, %0"
               : "=&s"(keep) : "v"(gsrc), "s"(lds_dst) : "memory");
}
__device__ __forceinline__ void glds16_s(const void* sbase, unsigned voff, unsigned lds_dst) {
  unsigned keep;
  asm volatile("s_mov_b32 %0, m0\n\ts_mov_b32 m0, %3\n\ts_nop 0\n\tglobal_load_lds_dwordx4 %1, %2\n\ts_mov_b32 m0, %0"
               : "=&s"(keep) : "v"(voff), "s"(sbase), "s"(lds_dst) : "memory");
}

__global__ __launch_bounds__(256) void ln_kernel(const u16* __restrict__ x, u16* __restrict__ xn) {
  const int wid = threadIdx.x >> 6, lane = threadIdx.x & 63;
  const int row = blockIdx.x * 4 + wid;
  const u16* xr = x + (size_t)row * D_;
  float v[16];
  {
    bf16x8 a = *(const bf16x8*)(xr + lane * 8);
    bf16x8 b = *(const bf16x8*)(xr + 512 + lane * 8);
#pragma unroll
    for (int j = 0; j < 8; ++j) { v[j] = bf2f((u16)a[j]); v[8 + j] = bf2f((u16)b[j]); }
  }
  float s = 0.f;
#pragma unroll
  for (int j = 0; j < 16; ++j) s += v[j];
#pragma unroll
  for (int o = 32; o >= 1; o >>= 1) s += __shfl_xor(s, o);
  const float mean = s * (1.0f / D_);
  float q = 0.f;
#pragma unroll
  for (int j = 0; j < 16; ++j) { float d = v[j] - mean; q += d * d; }
#pragma unroll
  for (int o = 32; o >= 1; o >>= 1) q += __shfl_xor(q, o);
  const float var = q * (1.0f / D_);
  const float mb = rbf(mean), vb = rbf(var);
  const float den = rbf(sqrtf(rbf(vb + 1e-5f)));
  bf16x8 a, b;
#pragma unroll
  for (int j = 0; j < 8; ++j) {
    a[j] = (short)f2bf(rbf(v[j] - mb) / den);
    b[j] = (short)f2bf(rbf(v[8 + j] - mb) / den);
  }
  u16* yr = xn + (size_t)row * D_;
  *(bf16x8*)(yr + lane * 8) = a;
  *(bf16x8*)(yr + 512 + lane * 8) = b;
}

constexpr int P1_A_BYTES = 256 * 64 * 2;
constexpr int P1_B_BYTES = 64 * 320 * 2;
constexpr int P1_STAGE = P1_A_BYTES + P1_B_BYTES;

__global__ __launch_bounds__(512, 2) void p1_kernel(const u16* __restrict__ xn, const u16* __restrict__ wqkv,
                                                    const u16* __restrict__ w1, const u16* __restrict__ w3,
                                                    u16* __restrict__ qkv, u16* __restrict__ hbuf) {
  __shared__ __attribute__((aligned(1024))) char smem[2 * P1_STAGE];
  const int tid = threadIdx.x, wid = tid >> 6, lane = tid & 63;
  const int wr = wid >> 2, wc = wid & 3;
  const int bid = blockIdx.x;
  const int ntile = (bid & 7) * 4 + ((bid >> 3) & 3);
  const int mtile = bid >> 5;
  const int m0 = mtile * 256;

  const char* abase = (const char*)(xn + (size_t)m0 * D_);
  const unsigned aofs = (unsigned)((tid >> 3) * D_ * 2 + (((tid & 7) ^ ((tid >> 3) & 7)) * 16));
  const u16* bsrc[5];
  bool bisq[5];
#pragma unroll
  for (int i = 0; i < 5; ++i) {
    const int p = i * 512 + tid, k = p / 40, g = p - k * 40;
    const int sw = ((k >> 1) & 1) | (((k >> 3) & 1) << 1);
    const int c = (g >> 1) ^ sw, n = c * 16 + (g & 1) * 8;
    bisq[i] = n < 96;
    if (n < 96) bsrc[i] = wqkv + (size_t)k * NQKV_ + ntile * 96 + n;
    else if (n < 208) bsrc[i] = w1 + (size_t)k * F_ + ntile * 112 + (n - 96);
    else bsrc[i] = w3 + (size_t)k * F_ + ntile * 112 + (n - 208);
  }

  const int fr = lane & 15, fq = lane >> 4;
  int aoff[2];
#pragma unroll
  for (int ks = 0; ks < 2; ++ks) aoff[ks] = (wr * 128 + fr) * 128 + (((4 * ks + fq) ^ (fr & 7)) * 16);
  int unit[5];
  if (wc < 3) { unit[0] = 6 + 2 * wc; unit[1] = 13 + 2 * wc; unit[2] = 7 + 2 * wc; unit[3] = 14 + 2 * wc; unit[4] = wc; }
  else { unit[0] = 12; unit[1] = 19; unit[2] = 3; unit[3] = 4; unit[4] = 5; }
  int boff[5];
  {
    const int q = fr >> 2, pp = fr & 3;
    const int sw = ((q >> 1) & 1) | ((fq & 1) << 1);
#pragma unroll
    for (int sidx = 0; sidx < 5; ++sidx) boff[sidx] = P1_A_BYTES + 640 * (8 * fq + q) + 8 * pp + 32 * (unit[sidx] ^ sw);
  }

  const int wid_s = __builtin_amdgcn_readfirstlane(wid);
  const unsigned lds0 = (unsigned)(size_t)(LDS_AS char*)smem + wid_s * 1024;
  f32x4 acc[5][8];
#pragma unroll
  for (int s = 0; s < 5; ++s)
#pragma unroll
    for (int m = 0; m < 8; ++m) acc[s][m] = (f32x4){0.f, 0.f, 0.f, 0.f};

  auto stage = [&](int buf, int kt) {
    const unsigned base = lds0 + buf * P1_STAGE;
#pragma unroll
    for (int i = 0; i < 4; ++i) glds16_s(abase + (i * 64 * D_ * 2 + kt * 128), aofs, base + i * 8192);
    const int koq = kt * 64 * NQKV_, kof = kt * 64 * F_;
#pragma unroll
    for (int i = 0; i < 5; ++i) glds16(bsrc[i] + (bisq[i] ? koq : kof), base + P1_A_BYTES + i * 8192);
  };

  stage(0, 0);
  WAIT_V0();
  __syncthreads();
  constexpr int NT = D_ / 64;
  for (int kt = 0; kt < NT; ++kt) {
    const int cur = kt & 1;
    if (kt + 1 < NT) stage(cur ^ 1, kt + 1);
    const char* sb = smem + cur * P1_STAGE;
#pragma unroll
    for (int ks = 0; ks < 2; ++ks) {
      bf16x8 af[8], bfr[5];
#pragma unroll
      for (int m = 0; m < 8; ++m) af[m] = *(const bf16x8*)(sb + aoff[ks] + m * 2048);
#pragma unroll
      for (int s = 0; s < 5; ++s) {
        s16x4 lo = tr_read(sb + boff[s] + ks * 20480);
        s16x4 hi = tr_read(sb + boff[s] + ks * 20480 + 2560);
        bfr[s] = (bf16x8){lo[0], lo[1], lo[2], lo[3], hi[0], hi[1], hi[2], hi[3]};
      }
#pragma unroll
      for (int s = 0; s < 5; ++s)
#pragma unroll
        for (int m = 0; m < 8; ++m)
          acc[s][m] = __builtin_amdgcn_mfma_f32_16x16x32_bf16(bfr[s], af[m], acc[s][m], 0, 0, 0);
    }
    WAIT_V0();
    __syncthreads();
  }

  {
    int elane = __builtin_amdgcn_mbcnt_hi(~0u, __builtin_amdgcn_mbcnt_lo(~0u, 0u));
    asm volatile("" : "+v"(elane));
    const int ewr = wid_s >> 2, ewc = wid_s & 3, efr = elane & 15, efq = elane >> 4;
    const int rowb = m0 + ewr * 128 + efr;
    u16* hp = hbuf + (size_t)rowb * F_ + ntile * 112 + 2 * ewc * 16 + 4 * efq;
    u16* qp = qkv + (size_t)rowb * NQKV_ + ntile * 96 + 4 * efq;
#define P1_STORE_QKV(SL, UNITQ)                                                              \
    _Pragma("unroll") for (int m = 0; m < 8; ++m) {                                          \
      u32x2 w;                                                                               \
      w.x = (unsigned)f2bf(acc[SL][m][0]) | ((unsigned)f2bf(acc[SL][m][1]) << 16);           \
      w.y = (unsigned)f2bf(acc[SL][m][2]) | ((unsigned)f2bf(acc[SL][m][3]) << 16);           \
      *(u32x2*)(qp + (size_t)(16 * m) * NQKV_ + (UNITQ) * 16) = w;                           \
      __builtin_amdgcn_sched_barrier(0);                                                     \
    }
#define P1_STORE_H(SL, COFF)                                                                 \
    _Pragma("unroll") for (int m = 0; m < 8; ++m) {                                          \
      u16 r[4];                                                                              \
      _Pragma("unroll") for (int j = 0; j < 4; ++j) {                                        \
        const float g = rbf(acc[SL][m][j]), u = rbf(acc[SL + 1][m][j]);                      \
        r[j] = f2bf(rbf(silu_f(g)) * u);                                                     \
      }                                                                                      \
      u32x2 w;                                                                               \
      w.x = (unsigned)r[0] | ((unsigned)r[1] << 16);                                         \
      w.y = (unsigned)r[2] | ((unsigned)r[3] << 16);                                         \
      *(u32x2*)(hp + (size_t)(16 * m) * F_ + (COFF)) = w;                                    \
      __builtin_amdgcn_sched_barrier(0);                                                     \
    }
    P1_STORE_H(0, 0)
    if (ewc < 3) { P1_STORE_H(2, 16) P1_STORE_QKV(4, ewc) }
    else { P1_STORE_QKV(2, 3) P1_STORE_QKV(3, 4) P1_STORE_QKV(4, 5) }
  }
}

__global__ __launch_bounds__(256) void attn_kernel(const u16* __restrict__ qkv, u16* __restrict__ o) {
  const int idx = blockIdx.x * 256 + threadIdx.x;
  const int s = idx >> 5, h = idx & 31;
  float q[32];
  {
    const u16* qp = qkv + (size_t)s * NQKV_ + h * DH_;
#pragma unroll
    for (int c = 0; c < 4; ++c) {
      bf16x8 t = *(const bf16x8*)(qp + c * 8);
#pragma unroll
      for (int j = 0; j < 8; ++j) q[c * 8 + j] = bf2f((u16)t[j]);
    }
  }
  float lg[8];
#pragma unroll
  for (int w = 0; w < 8; ++w) {
    const int r = s - 2 * w;
    if (r >= 0) {
      const u16* kp = qkv + (size_t)r * NQKV_ + D_ + h * DH_;
      float d = 0.f;
#pragma unroll
      for (int c = 0; c < 4; ++c) {
        bf16x8 t = *(const bf16x8*)(kp + c * 8);
#pragma unroll
        for (int j = 0; j < 8; ++j) d += q[c * 8 + j] * bf2f((u16)t[j]);
      }
      lg[w] = rbf(rbf(d) * 0.17677669529663687f);
    } else {
      lg[w] = rbf(-1e9f);
    }
  }
  float mx = lg[0];
#pragma unroll
  for (int w = 1; w < 8; ++w) mx = fmaxf(mx, lg[w]);
  float e[8], sum = 0.f;
#pragma unroll
  for (int w = 0; w < 8; ++w) { e[w] = __expf(lg[w] - mx); sum += e[w]; }
  const float inv = 1.0f / sum;
  float acc[32];
#pragma unroll
  for (int j = 0; j < 32; ++j) acc[j] = 0.f;
#pragma unroll
  for (int w = 0; w < 8; ++w) {
    const int r = s - 2 * w;
    if (r >= 0) {
      const float p = rbf(e[w] * inv);
      const u16* vp = qkv + (size_t)r * NQKV_ + 2 * D_ + h * DH_;
#pragma unroll
      for (int c = 0; c < 4; ++c) {
        bf16x8 t = *(const bf16x8*)(vp + c * 8);
#pragma unroll
        for (int j = 0; j < 8; ++j) acc[c * 8 + j] += p * bf2f((u16)t[j]);
      }
    }
  }
  u16* op = o + (size_t)s * D_ + h * DH_;
#pragma unroll
  for (int c = 0; c < 4; ++c) {
    bf16x8 t;
#pragma unroll
    for (int j = 0; j < 8; ++j) t[j] = (short)f2bf(acc[c * 8 + j]);
    *(bf16x8*)(op + c * 8) = t;
  }
}

constexpr int P2_BM = 128, P2_BN = 128;
constexpr int P2_A_BYTES = P2_BM * 64 * 2;
constexpr int P2_B_BYTES = 64 * P2_BN * 2;
constexpr int P2_STAGE = P2_A_BYTES + P2_B_BYTES;

__global__ __launch_bounds__(256, 2) void p2_kernel(const u16* __restrict__ obuf, const u16* __restrict__ hbuf,
                                                    const u16* __restrict__ wo, const u16* __restrict__ w2,
                                                    const u16* __restrict__ x, u16* __restrict__ out) {
  __shared__ __attribute__((aligned(1024))) char smem[2 * P2_STAGE];
  const int tid = threadIdx.x, wid = tid >> 6, lane = tid & 63;
  const int wr = wid >> 1, wc = wid & 1;
  const int nNt = D_ / P2_BN;
  const int mtile = blockIdx.x / nNt, ntile = blockIdx.x % nNt;
  const int m0 = mtile * P2_BM, n0 = ntile * P2_BN;

  int arow[4], acol[4];
#pragma unroll
  for (int i = 0; i < 4; ++i) {
    const int p = i * 256 + tid, row = p >> 3;
    arow[i] = m0 + row; acol[i] = ((p & 7) ^ (row & 7)) * 8;
  }
  int bk[4], bn[4];
#pragma unroll
  for (int i = 0; i < 4; ++i) {
    const int p = i * 256 + tid, k = p >> 4, g = p & 15;
    const int sw = (k & 3) | (((k >> 3) & 1) << 2);
    bk[i] = k; bn[i] = n0 + ((g >> 1) ^ sw) * 16 + (g & 1) * 8;
  }
  const int fr = lane & 15, fq = lane >> 4;
  int aoff[2];
#pragma unroll
  for (int ks = 0; ks < 2; ++ks) aoff[ks] = (wr * 64 + fr) * 128 + (((4 * ks + fq) ^ (fr & 7)) * 16);
  int boff[4];
  {
    const int q = fr >> 2, pp = fr & 3;
    const int sw = q | ((fq & 1) << 2);
#pragma unroll
    for (int n = 0; n < 4; ++n) boff[n] = P2_A_BYTES + 256 * (8 * fq + q) + 8 * pp + 32 * ((wc * 4 + n) ^ sw);
  }

  f32x4 accs[4][4], acco[4][4];
#pragma unroll
  for (int n = 0; n < 4; ++n)
#pragma unroll
    for (int m = 0; m < 4; ++m) { accs[n][m] = (f32x4){0.f, 0.f, 0.f, 0.f}; acco[n][m] = (f32x4){0.f, 0.f, 0.f, 0.f}; }

  constexpr int NT1 = D_ / 64, NT2 = F_ / 64, NT = NT1 + NT2;
  auto stage = [&](int buf, int kt) {
    char* base = smem + buf * P2_STAGE + wid * 1024;
    if (kt < NT1) {
#pragma unroll
      for (int i = 0; i < 4; ++i)
        __builtin_amdgcn_global_load_lds((const unsigned*)(obuf + (size_t)arow[i] * D_ + kt * 64 + acol[i]), (LDS_AS unsigned*)(base + i * 4096), 16, 0, 0);
#pragma unroll
      for (int i = 0; i < 4; ++i)
        __builtin_amdgcn_global_load_lds((const unsigned*)(wo + (size_t)(kt * 64 + bk[i]) * D_ + bn[i]), (LDS_AS unsigned*)(base + P2_A_BYTES + i * 4096), 16, 0, 0);
    } else {
      const int k2 = kt - NT1;
#pragma unroll
      for (int i = 0; i < 4; ++i)
        __builtin_amdgcn_global_load_lds((const unsigned*)(hbuf + (size_t)arow[i] * F_ + k2 * 64 + acol[i]), (LDS_AS unsigned*)(base + i * 4096), 16, 0, 0);
#pragma unroll
      for (int i = 0; i < 4; ++i)
        __builtin_amdgcn_global_load_lds((const unsigned*)(w2 + (size_t)(k2 * 64 + bk[i]) * D_ + bn[i]), (LDS_AS unsigned*)(base + P2_A_BYTES + i * 4096), 16, 0, 0);
    }
  };

  stage(0, 0);
  WAIT_V0();
  __syncthreads();
  for (int kt = 0; kt < NT1; ++kt) {
    const int cur = kt & 1;
    stage(cur ^ 1, kt + 1);
    const char* sb = smem + cur * P2_STAGE;
#pragma unroll
    for (int ks = 0; ks < 2; ++ks) {
      bf16x8 af[4], bfr[4];
#pragma unroll
      for (int m = 0; m < 4; ++m) af[m] = *(const bf16x8*)(sb + aoff[ks] + m * 2048);
#pragma unroll
      for (int n = 0; n < 4; ++n) {
        s16x4 lo = tr_read(sb + boff[n] + ks * 8192);
        s16x4 hi = tr_read(sb + boff[n] + ks * 8192 + 1024);
        bfr[n] = (bf16x8){lo[0], lo[1], lo[2], lo[3], hi[0], hi[1], hi[2], hi[3]};
      }
#pragma unroll
      for (int n = 0; n < 4; ++n)
#pragma unroll
        for (int m = 0; m < 4; ++m) accs[n][m] = __builtin_amdgcn_mfma_f32_16x16x32_bf16(bfr[n], af[m], accs[n][m], 0, 0, 0);
    }
    WAIT_V0();
    __syncthreads();
  }
  for (int kt = NT1; kt < NT; ++kt) {
    const int cur = kt & 1;
    if (kt + 1 < NT) stage(cur ^ 1, kt + 1);
    const char* sb = smem + cur * P2_STAGE;
#pragma unroll
    for (int ks = 0; ks < 2; ++ks) {
      bf16x8 af[4], bfr[4];
#pragma unroll
      for (int m = 0; m < 4; ++m) af[m] = *(const bf16x8*)(sb + aoff[ks] + m * 2048);
#pragma unroll
      for (int n = 0; n < 4; ++n) {
        s16x4 lo = tr_read(sb + boff[n] + ks * 8192);
        s16x4 hi = tr_read(sb + boff[n] + ks * 8192 + 1024);
        bfr[n] = (bf16x8){lo[0], lo[1], lo[2], lo[3], hi[0], hi[1], hi[2], hi[3]};
      }
#pragma unroll
      for (int n = 0; n < 4; ++n)
#pragma unroll
        for (int m = 0; m < 4; ++m) acco[n][m] = __builtin_amdgcn_mfma_f32_16x16x32_bf16(bfr[n], af[m], acco[n][m], 0, 0, 0);
    }
    WAIT_V0();
    __syncthreads();
  }

#pragma unroll
  for (int n = 0; n < 4; ++n)
#pragma unroll
    for (int m = 0; m < 4; ++m) {
      const size_t off = (size_t)(m0 + wr * 64 + 16 * m + fr) * D_ + n0 + (wc * 4 + n) * 16 + 4 * fq;
      const u32x2 xv = *(const u32x2*)(x + off);
      u16 r[4];
#pragma unroll
      for (int j = 0; j < 4; ++j) {
        const unsigned xw = (j < 2) ? xv.x : xv.y;
        const float xf = bf2f((u16)((j & 1) ? (xw >> 16) : (xw & 0xffff)));
        const float sv = rbf(accs[n][m][j]);
        const float g = rbf(silu_f(rbf(acco[n][m][j])));
        r[j] = f2bf(xf + rbf(sv * g));
      }
      u32x2 w;
      w.x = (unsigned)r[0] | ((unsigned)r[1] << 16);
      w.y = (unsigned)r[2] | ((unsigned)r[3] << 16);
      *(u32x2*)(out + off) = w;
    }
}

extern "C" void kernel_launch(void* const* d_in, const int* in_sizes, int n_in, void* d_out, int out_size, void* d_ws,
                              size_t ws_size, hipStream_t stream) {
  const u16* x = (const u16*)d_in[0];
  const u16* wqkv = (const u16*)d_in[1];
  const u16* wo = (const u16*)d_in[2];
  const u16* w1 = (const u16*)d_in[4];
  const u16* w3 = (const u16*)d_in[5];
  const u16* w2 = (const u16*)d_in[6];
  u16* out = (u16*)d_out;
  char* ws = (char*)d_ws;
  u16* xn = (u16*)ws;
  u16* qkv = (u16*)(ws + (size_t)S_ * D_ * 2);
  u16* hbuf = qkv + (size_t)S_ * NQKV_;
  u16* obuf = hbuf + (size_t)S_ * F_;

  ln_kernel<<<S_ / 4, 256, 0, stream>>>(x, xn);
  p1_kernel<<<256, 512, 0, stream>>>(xn, wqkv, w1, w3, qkv, hbuf);
  attn_kernel<<<S_ * H_ / 256, 256, 0, stream>>>(qkv, obuf);
  p2_kernel<<<(S_ / P2_BM) * (D_ / P2_BN), 256, 0, stream>>>(obuf, hbuf, wo, w2, x, out);
}
```

```cpp
#include <hip/hip_runtime.h>
#include <hip/hip_bf16.h>
#include <stdint.h>

typedef unsigned short u16;
typedef short bf16x8 __attribute__((ext_vector_type(8)));
typedef short s16x4 __attribute__((ext_vector_type(4)));
typedef float f32x4 __attribute__((ext_vector_type(4)));
typedef unsigned u32x2 __attribute__((ext_vector_type(2)));
typedef unsigned u32x4 __attribute__((ext_vector_type(4)));

#define LDS_AS __attribute__((address_space(3)))
#define WAIT_V0() asm volatile("s_waitcnt vmcnt(0)" ::: "memory")

constexpr int S_ = 2048, D_ = 1024, F_ = 3584, H_ = 32, DH_ = 32, NQKV_ = 3072;

__device__ __forceinline__ float bf2f(u16 b) { return __uint_as_float(((uint32_t)b) << 16); }
__device__ __forceinline__ u16 f2bf(float f) {
  __bf16 b = (__bf16)f;
  return __builtin_bit_cast(u16, b);
}
__device__ __forceinline__ float rbf(float f) { return bf2f(f2bf(f)); }
__device__ __forceinline__ float silu_f(float v) { return v / (1.0f + __expf(-v)); }

__device__ __forceinline__ s16x4 tr_read(const char* lds_ptr) {
  return __builtin_amdgcn_ds_read_tr16_b64_v4i16((LDS_AS s16x4*)(lds_ptr));
}

__device__ __forceinline__ void glds16(const void* gsrc, unsigned lds_dst) {
  unsigned keep;
  asm volatile("s_mov_b32 %0, m0\n\ts_mov_b32 m0, %2\n\ts_nop 0\n\tglobal_load_lds_dwordx4 %1, off\n\ts_mov_b32 m0, %0"
               : "=&s"(keep) : "v"(gsrc), "s"(lds_dst) : "memory");
}
__device__ __forceinline__ void glds16_s(const void* sbase, unsigned voff, unsigned lds_dst) {
  unsigned keep;
  asm volatile("s_mov_b32 %0, m0\n\ts_mov_b32 m0, %3\n\ts_nop 0\n\tglobal_load_lds_dwordx4 %1, %2\n\ts_mov_b32 m0, %0"
               : "=&s"(keep) : "v"(voff), "s"(sbase), "s"(lds_dst) : "memory");
}

__global__ __launch_bounds__(256) void ln_kernel(const u16* __restrict__ x, u16* __restrict__ xn, unsigned* __restrict__ cnt) {
  if (blockIdx.x == 0 && threadIdx.x < 64) cnt[threadIdx.x] = 0u;
  const int wid = threadIdx.x >> 6, lane = threadIdx.x & 63;
  const int row = blockIdx.x * 4 + wid;
  const u16* xr = x + (size_t)row * D_;
  float v[16];
  {
    bf16x8 a = *(const bf16x8*)(xr + lane * 8);
    bf16x8 b = *(const bf16x8*)(xr + 512 + lane * 8);
#pragma unroll
    for (int j = 0; j < 8; ++j) { v[j] = bf2f((u16)a[j]); v[8 + j] = bf2f((u16)b[j]); }
  }
  float s = 0.f;
#pragma unroll
  for (int j = 0; j < 16; ++j) s += v[j];
#pragma unroll
  for (int o = 32; o >= 1; o >>= 1) s += __shfl_xor(s, o);
  const float mean = s * (1.0f / D_);
  float q = 0.f;
#pragma unroll
  for (int j = 0; j < 16; ++j) { float d = v[j] - mean; q += d * d; }
#pragma unroll
  for (int o = 32; o >= 1; o >>= 1) q += __shfl_xor(q, o);
  const float var = q * (1.0f / D_);
  const float mb = rbf(mean), vb = rbf(var);
  const float den = rbf(sqrtf(rbf(vb + 1e-5f)));
  bf16x8 a, b;
#pragma unroll
  for (int j = 0; j < 8; ++j) {
    a[j] = (short)f2bf(rbf(v[j] - mb) / den);
    b[j] = (short)f2bf(rbf(v[8 + j] - mb) / den);
  }
  u16* yr = xn + (size_t)row * D_;
  *(bf16x8*)(yr + lane * 8) = a;
  *(bf16x8*)(yr + 512 + lane * 8) = b;
}

constexpr int P1_A_BYTES = 256 * 64 * 2;
constexpr int P1_B_BYTES = 64 * 320 * 2;
constexpr int P1_STAGE = P1_A_BYTES + P1_B_BYTES;

__global__ __launch_bounds__(512, 2) void p1_kernel(const u16* __restrict__ xn, const u16* __restrict__ wqkv,
                                                    const u16* __restrict__ w1, const u16* __restrict__ w3,
                                                    u16* __restrict__ qkv, u16* __restrict__ hbuf) {
  __shared__ __attribute__((aligned(1024))) char smem[2 * P1_STAGE];
  const int tid = threadIdx.x, wid = tid >> 6, lane = tid & 63;
  const int wr = wid >> 2, wc = wid & 3;
  const int bid = blockIdx.x;
  const int ntile = (bid & 7) * 4 + ((bid >> 3) & 3);
  const int mtile = bid >> 5;
  const int m0 = mtile * 256;

  const char* abase = (const char*)(xn + (size_t)m0 * D_);
  const unsigned aofs = (unsigned)((tid >> 3) * D_ * 2 + (((tid & 7) ^ ((tid >> 3) & 7)) * 16));
  const u16* bsrc[5];
  bool bisq[5];
#pragma unroll
  for (int i = 0; i < 5; ++i) {
    const int p = i * 512 + tid, k = p / 40, g = p - k * 40;
    const int sw = ((k >> 1) & 1) | (((k >> 3) & 1) << 1);
    const int c = (g >> 1) ^ sw, n = c * 16 + (g & 1) * 8;
    bisq[i] = n < 96;
    if (n < 96) bsrc[i] = wqkv + (size_t)k * NQKV_ + ntile * 96 + n;
    else if (n < 208) bsrc[i] = w1 + (size_t)k * F_ + ntile * 112 + (n - 96);
    else bsrc[i] = w3 + (size_t)k * F_ + ntile * 112 + (n - 208);
  }

  const int fr = lane & 15, fq = lane >> 4;
  int aoff[2];
#pragma unroll
  for (int ks = 0; ks < 2; ++ks) aoff[ks] = (wr * 128 + fr) * 128 + (((4 * ks + fq) ^ (fr & 7)) * 16);
  int unit[5];
  if (wc < 3) { unit[0] = 6 + 2 * wc; unit[1] = 13 + 2 * wc; unit[2] = 7 + 2 * wc; unit[3] = 14 + 2 * wc; unit[4] = wc; }
  else { unit[0] = 12; unit[1] = 19; unit[2] = 3; unit[3] = 4; unit[4] = 5; }
  int boff[5];
  {
    const int q = fr >> 2, pp = fr & 3;
    const int sw = ((q >> 1) & 1) | ((fq & 1) << 1);
#pragma unroll
    for (int sidx = 0; sidx < 5; ++sidx) boff[sidx] = P1_A_BYTES + 640 * (8 * fq + q) + 8 * pp + 32 * (unit[sidx] ^ sw);
  }

  const int wid_s = __builtin_amdgcn_readfirstlane(wid);
  const unsigned lds0 = (unsigned)(size_t)(LDS_AS char*)smem + wid_s * 1024;
  f32x4 acc[5][8];
#pragma unroll
  for (int s = 0; s < 5; ++s)
#pragma unroll
    for (int m = 0; m < 8; ++m) acc[s][m] = (f32x4){0.f, 0.f, 0.f, 0.f};

  auto stage = [&](int buf, int kt) {
    const unsigned base = lds0 + buf * P1_STAGE;
#pragma unroll
    for (int i = 0; i < 4; ++i) glds16_s(abase + (i * 64 * D_ * 2 + kt * 128), aofs, base + i * 8192);
    const int koq = kt * 64 * NQKV_, kof = kt * 64 * F_;
#pragma unroll
    for (int i = 0; i < 5; ++i) glds16(bsrc[i] + (bisq[i] ? koq : kof), base + P1_A_BYTES + i * 8192);
  };

  stage(0, 0);
  WAIT_V0();
  __syncthreads();
  constexpr int NT = D_ / 64;
  for (int kt = 0; kt < NT; ++kt) {
    const int cur = kt & 1;
    if (kt + 1 < NT) stage(cur ^ 1, kt + 1);
    const char* sb = smem + cur * P1_STAGE;
#pragma unroll
    for (int ks = 0; ks < 2; ++ks) {
      bf16x8 af[8], bfr[5];
#pragma unroll
      for (int m = 0; m < 8; ++m) af[m] = *(const bf16x8*)(sb + aoff[ks] + m * 2048);
#pragma unroll
      for (int s = 0; s < 5; ++s) {
        s16x4 lo = tr_read(sb + boff[s] + ks * 20480);
        s16x4 hi = tr_read(sb + boff[s] + ks * 20480 + 2560);
        bfr[s] = (bf16x8){lo[0], lo[1], lo[2], lo[3], hi[0], hi[1], hi[2], hi[3]};
      }
#pragma unroll
      for (int s = 0; s < 5; ++s)
#pragma unroll
        for (int m = 0; m < 8; ++m)
          acc[s][m] = __builtin_amdgcn_mfma_f32_16x16x32_bf16(bfr[s], af[m], acc[s][m], 0, 0, 0);
    }
    WAIT_V0();
    __syncthreads();
  }

  {
    int elane = __builtin_amdgcn_mbcnt_hi(~0u, __builtin_amdgcn_mbcnt_lo(~0u, 0u));
    asm volatile("" : "+v"(elane));
    const int ewr = wid_s >> 2, ewc = wid_s & 3, efr = elane & 15, efq = elane >> 4;
    const int rowb = m0 + ewr * 128 + efr;
    u16* hp = hbuf + (size_t)rowb * F_ + ntile * 112 + 2 * ewc * 16 + 4 * efq;
    u16* qp = qkv + (size_t)rowb * NQKV_ + ntile * 96 + 4 * efq;
#define P1_STORE_QKV(SL, UNITQ)                                                              \
    _Pragma("unroll") for (int m = 0; m < 8; ++m) {                                          \
      u32x2 w;                                                                               \
      w.x = (unsigned)f2bf(acc[SL][m][0]) | ((unsigned)f2bf(acc[SL][m][1]) << 16);           \
      w.y = (unsigned)f2bf(acc[SL][m][2]) | ((unsigned)f2bf(acc[SL][m][3]) << 16);           \
      *(u32x2*)(qp + (size_t)(16 * m) * NQKV_ + (UNITQ) * 16) = w;                           \
      __builtin_amdgcn_sched_barrier(0);                                                     \
    }
#define P1_STORE_H(SL, COFF)                                                                 \
    _Pragma("unroll") for (int m = 0; m < 8; ++m) {                                          \
      u16 r[4];                                                                              \
      _Pragma("unroll") for (int j = 0; j < 4; ++j) {                                        \
        const float g = rbf(acc[SL][m][j]), u = rbf(acc[SL + 1][m][j]);                      \
        r[j] = f2bf(rbf(silu_f(g)) * u);                                                     \
      }                                                                                      \
      u32x2 w;                                                                               \
      w.x = (unsigned)r[0] | ((unsigned)r[1] << 16);                                         \
      w.y = (unsigned)r[2] | ((unsigned)r[3] << 16);                                         \
      *(u32x2*)(hp + (size_t)(16 * m) * F_ + (COFF)) = w;                                    \
      __builtin_amdgcn_sched_barrier(0);                                                     \
    }
    P1_STORE_H(0, 0)
    if (ewc < 3) { P1_STORE_H(2, 16) P1_STORE_QKV(4, ewc) }
    else { P1_STORE_QKV(2, 3) P1_STORE_QKV(3, 4) P1_STORE_QKV(4, 5) }
  }
}

__global__ __launch_bounds__(256) void attn_kernel(const u16* __restrict__ qkv, u16* __restrict__ o) {
  const int idx = blockIdx.x * 256 + threadIdx.x;
  const int s = idx >> 5, h = idx & 31;
  float q[32];
  {
    const u16* qp = qkv + (size_t)s * NQKV_ + h * DH_;
#pragma unroll
    for (int c = 0; c < 4; ++c) {
      bf16x8 t = *(const bf16x8*)(qp + c * 8);
#pragma unroll
      for (int j = 0; j < 8; ++j) q[c * 8 + j] = bf2f((u16)t[j]);
    }
  }
  float lg[8];
#pragma unroll
  for (int w = 0; w < 8; ++w) {
    const int r = s - 2 * w;
    if (r >= 0) {
      const u16* kp = qkv + (size_t)r * NQKV_ + D_ + h * DH_;
      float d = 0.f;
#pragma unroll
      for (int c = 0; c < 4; ++c) {
        bf16x8 t = *(const bf16x8*)(kp + c * 8);
#pragma unroll
        for (int j = 0; j < 8; ++j) d += q[c * 8 + j] * bf2f((u16)t[j]);
      }
      lg[w] = rbf(rbf(d) * 0.17677669529663687f);
    } else {
      lg[w] = rbf(-1e9f);
    }
  }
  float mx = lg[0];
#pragma unroll
  for (int w = 1; w < 8; ++w) mx = fmaxf(mx, lg[w]);
  float e[8], sum = 0.f;
#pragma unroll
  for (int w = 0; w < 8; ++w) { e[w] = __expf(lg[w] - mx); sum += e[w]; }
  const float inv = 1.0f / sum;
  float acc[32];
#pragma unroll
  for (int j = 0; j < 32; ++j) acc[j] = 0.f;
#pragma unroll
  for (int w = 0; w < 8; ++w) {
    const int r = s - 2 * w;
    if (r >= 0) {
      const float p = rbf(e[w] * inv);
      const u16* vp = qkv + (size_t)r * NQKV_ + 2 * D_ + h * DH_;
#pragma unroll
      for (int c = 0; c < 4; ++c) {
        bf16x8 t = *(const bf16x8*)(vp + c * 8);
#pragma unroll
        for (int j = 0; j < 8; ++j) acc[c * 8 + j] += p * bf2f((u16)t[j]);
      }
    }
  }
  u16* op = o + (size_t)s * D_ + h * DH_;
#pragma unroll
  for (int c = 0; c < 4; ++c) {
    bf16x8 t;
#pragma unroll
    for (int j = 0; j < 8; ++j) t[j] = (short)f2bf(acc[c * 8 + j]);
    *(bf16x8*)(op + c * 8) = t;
  }
}

constexpr int P2_A_BYTES = 256 * 64 * 2;
constexpr int P2_B_BYTES = 64 * 128 * 2;
constexpr int P2_STAGE = P2_A_BYTES + P2_B_BYTES;
constexpr int P2_NSTAGE = 3;

__global__ __launch_bounds__(512, 2) void p2_kernel(const u16* __restrict__ obuf, const u16* __restrict__ hbuf,
                                                    const u16* __restrict__ wo, const u16* __restrict__ w2,
                                                    const u16* __restrict__ x, u16* __restrict__ out,
                                                    float* __restrict__ part, unsigned* __restrict__ cnt) {
  __shared__ __attribute__((aligned(1024))) char smem[P2_NSTAGE * P2_STAGE];
  const int tid = threadIdx.x, wid = tid >> 6, lane = tid & 63;
  const int wr = wid >> 1, wc = wid & 1;
  const int bid = blockIdx.x;
  const int xcd = bid & 7, idx = bid >> 3, split = idx & 3, tt = idx >> 2;
  const int mtile = (xcd >> 1) * 2 + (tt & 1), ntile = (xcd & 1) * 4 + (tt >> 1);
  const int tile = mtile * 8 + ntile;
  const int m0 = mtile * 256, n0 = ntile * 128;

  const unsigned arow = tid >> 3, achunk = ((tid & 7) ^ ((tid >> 3) & 7)) * 16;
  const unsigned aofs_o = arow * (D_ * 2) + achunk, aofs_h = arow * (F_ * 2) + achunk;
  const char* abase_o = (const char*)(obuf + (size_t)m0 * D_);
  const char* abase_h = (const char*)(hbuf + (size_t)m0 * F_);
  const unsigned bkk = tid >> 4;
  const unsigned bsw = (bkk & 3) | (((bkk >> 3) & 1) << 2);
  const unsigned bofs = bkk * (D_ * 2) + ((((tid & 15) >> 1) ^ bsw) * 32) + (tid & 1) * 16;
  const char* bbase_o = (const char*)(wo + n0);
  const char* bbase_h = (const char*)(w2 + n0);

  const int fr = lane & 15, fq = lane >> 4;
  int aoff[2];
#pragma unroll
  for (int ks = 0; ks < 2; ++ks) aoff[ks] = (wr * 64 + fr) * 128 + (((4 * ks + fq) ^ (fr & 7)) * 16);
  int boff[4];
  {
    const int q = fr >> 2, pp = fr & 3;
    const int sw = q | ((fq & 1) << 2);
#pragma unroll
    for (int n = 0; n < 4; ++n) boff[n] = P2_A_BYTES + 256 * (8 * fq + q) + 8 * pp + 32 * ((wc * 4 + n) ^ sw);
  }

  const int wid_s = __builtin_amdgcn_readfirstlane(wid);
  const unsigned lds0 = (unsigned)(size_t)(LDS_AS char*)smem + wid_s * 1024;
  f32x4 accs[4][4], acco[4][4];
#pragma unroll
  for (int n = 0; n < 4; ++n)
#pragma unroll
    for (int m = 0; m < 4; ++m) { accs[n][m] = (f32x4){0.f, 0.f, 0.f, 0.f}; acco[n][m] = (f32x4){0.f, 0.f, 0.f, 0.f}; }

  const int nq = (split == 0) ? 16 : 0;
  const int k2base = (split == 0) ? -16 : 2 + 18 * (split - 1);
  auto stage = [&](int buf, int t) {
    const unsigned base = lds0 + buf * P2_STAGE;
    if (t < nq) {
#pragma unroll
      for (int i = 0; i < 4; ++i) glds16_s(abase_o + (i * 64 * D_ * 2 + t * 128), aofs_o, base + i * 8192);
#pragma unroll
      for (int i = 0; i < 2; ++i) glds16_s(bbase_o + (size_t)(t * 64 + i * 32) * D_ * 2, bofs, base + P2_A_BYTES + i * 8192);
    } else {
      const int k2 = k2base + t;
#pragma unroll
      for (int i = 0; i < 4; ++i) glds16_s(abase_h + (i * 64 * F_ * 2 + k2 * 128), aofs_h, base + i * 8192);
#pragma unroll
      for (int i = 0; i < 2; ++i) glds16_s(bbase_h + (size_t)(k2 * 64 + i * 32) * D_ * 2, bofs, base + P2_A_BYTES + i * 8192);
    }
  };
#define P2_COMPUTE(ACC)                                                                               \
  {                                                                                                   \
    const char* sb = smem + cur * P2_STAGE;                                                           \
    _Pragma("unroll") for (int ks = 0; ks < 2; ++ks) {                                                \
      bf16x8 af[4], bfr[4];                                                                           \
      _Pragma("unroll") for (int m = 0; m < 4; ++m) af[m] = *(const bf16x8*)(sb + aoff[ks] + m * 2048); \
      _Pragma("unroll") for (int n = 0; n < 4; ++n) {                                                 \
        s16x4 lo = tr_read(sb + boff[n] + ks * 8192);                                                 \
        s16x4 hi = tr_read(sb + boff[n] + ks * 8192 + 1024);                                          \
        bfr[n] = (bf16x8){lo[0], lo[1], lo[2], lo[3], hi[0], hi[1], hi[2], hi[3]};                    \
      }                                                                                               \
      _Pragma("unroll") for (int n = 0; n < 4; ++n)                                                   \
        _Pragma("unroll") for (int m = 0; m < 4; ++m)                                                 \
          ACC[n][m] = __builtin_amdgcn_mfma_f32_16x16x32_bf16(bfr[n], af[m], ACC[n][m], 0, 0, 0);     \
    }                                                                                                 \
  }

  constexpr int NT = 18;
  stage(0, 0);
  stage(1, 1);
  int cur = 0, nxt = 2;
  for (int t = 0; t < nq; ++t) {
    asm volatile("s_waitcnt vmcnt(6)" ::: "memory");
    __syncthreads();
    stage(nxt, t + 2);
    P2_COMPUTE(accs)
    cur = (cur == 2) ? 0 : cur + 1;
    nxt = (nxt == 2) ? 0 : nxt + 1;
  }
  for (int t = nq; t < NT - 2; ++t) {
    asm volatile("s_waitcnt vmcnt(6)" ::: "memory");
    __syncthreads();
    stage(nxt, t + 2);
    P2_COMPUTE(acco)
    cur = (cur == 2) ? 0 : cur + 1;
    nxt = (nxt == 2) ? 0 : nxt + 1;
  }
  asm volatile("s_waitcnt vmcnt(6)" ::: "memory");
  __syncthreads();
  P2_COMPUTE(acco)
  cur = (cur == 2) ? 0 : cur + 1;
  asm volatile("s_waitcnt vmcnt(0)" ::: "memory");
  __syncthreads();
  P2_COMPUTE(acco)

  const auto rs = __builtin_amdgcn_make_buffer_rsrc(part, 0, 64u * 20u * 32768u, 0x00020000);
  const unsigned tbase = (unsigned)tile * 20u * 32768u + (unsigned)tid * 16u;
#pragma unroll
  for (int m = 0; m < 4; ++m) {
    if (m != split) {
#pragma unroll
      for (int n = 0; n < 4; ++n)
        __builtin_amdgcn_raw_buffer_store_b128(__builtin_bit_cast(u32x4, acco[n][m]), rs, tbase + (split * 4 + m) * 32768u + n * 8192u, 0, 16);
      if (split == 0) {
#pragma unroll
        for (int n = 0; n < 4; ++n)
          __builtin_amdgcn_raw_buffer_store_b128(__builtin_bit_cast(u32x4, accs[n][m]), rs, tbase + (16 + m) * 32768u + n * 8192u, 0, 16);
      }
    }
  }
  asm volatile("s_waitcnt vmcnt(0)" ::: "memory");
  __syncthreads();
  if (tid == 0) {
    __hip_atomic_fetch_add(cnt + tile, 1u, __ATOMIC_RELAXED, __HIP_MEMORY_SCOPE_AGENT);
    while (__hip_atomic_load(cnt + tile, __ATOMIC_RELAXED, __HIP_MEMORY_SCOPE_AGENT) < 4u) __builtin_amdgcn_s_sleep(1);
  }
  __syncthreads();
  asm volatile("" ::: "memory");
  f32x4 fo[4], fs[4];
#pragma unroll
  for (int n = 0; n < 4; ++n) {
    fo[n] = split == 0 ? acco[n][0] : split == 1 ? acco[n][1] : split == 2 ? acco[n][2] : acco[n][3];
    fs[n] = accs[n][0];
  }
#pragma unroll
  for (int src = 0; src < 4; ++src) {
    if (src != split) {
#pragma unroll
      for (int n = 0; n < 4; ++n)
        fo[n] += __builtin_bit_cast(f32x4, __builtin_amdgcn_raw_buffer_load_b128(rs, tbase + (src * 4 + split) * 32768u + n * 8192u, 0, 16));
    }
  }
  if (split != 0) {
#pragma unroll
    for (int n = 0; n < 4; ++n)
      fs[n] = __builtin_bit_cast(f32x4, __builtin_amdgcn_raw_buffer_load_b128(rs, tbase + (16 + split) * 32768u + n * 8192u, 0, 16));
  }
#pragma unroll
  for (int n = 0; n < 4; ++n) {
    const size_t off = (size_t)(m0 + wr * 64 + 16 * split + fr) * D_ + n0 + (wc * 4 + n) * 16 + 4 * fq;
    const u32x2 xv = *(const u32x2*)(x + off);
    u16 r[4];
#pragma unroll
    for (int j = 0; j < 4; ++j) {
      const unsigned xw = (j < 2) ? xv.x : xv.y;
      const float xf = bf2f((u16)((j & 1) ? (xw >> 16) : (xw & 0xffff)));
      const float sv = rbf(fs[n][j]);
      const float g = rbf(silu_f(rbf(fo[n][j])));
      r[j] = f2bf(xf + rbf(sv * g));
    }
    u32x2 w;
    w.x = (unsigned)r[0] | ((unsigned)r[1] << 16);
    w.y = (unsigned)r[2] | ((unsigned)r[3] << 16);
    *(u32x2*)(out + off) = w;
  }
}

extern "C" void kernel_launch(void* const* d_in, const int* in_sizes, int n_in, void* d_out, int out_size, void* d_ws,
                              size_t ws_size, hipStream_t stream) {
  const u16* x = (const u16*)d_in[0];
  const u16* wqkv = (const u16*)d_in[1];
  const u16* wo = (const u16*)d_in[2];
  const u16* w1 = (const u16*)d_in[4];
  const u16* w3 = (const u16*)d_in[5];
  const u16* w2 = (const u16*)d_in[6];
  u16* out = (u16*)d_out;
  char* ws = (char*)d_ws;
  u16* xn = (u16*)ws;
  u16* qkv = (u16*)(ws + (size_t)S_ * D_ * 2);
  u16* hbuf = qkv + (size_t)S_ * NQKV_;
  u16* obuf = hbuf + (size_t)S_ * F_;
  float* part = (float*)(obuf + (size_t)S_ * D_);
  unsigned* cnt = (unsigned*)((char*)part + (size_t)64 * 20 * 32768);

  ln_kernel<<<S_ / 4, 256, 0, stream>>>(x, xn, cnt);
  p1_kernel<<<256, 512, 0, stream>>>(xn, wqkv, w1, w3, qkv, hbuf);
  attn_kernel<<<S_ * H_ / 256, 256, 0, stream>>>(qkv, obuf);
  p2_kernel<<<256, 512, 0, stream>>>(obuf, hbuf, wo, w2, x, out, part, cnt);
}
```

```cpp
#include <hip/hip_runtime.h>
#include <hip/hip_bf16.h>
#include <stdint.h>

typedef unsigned short u16;
typedef short bf16x8 __attribute__((ext_vector_type(8)));
typedef short s16x4 __attribute__((ext_vector_type(4)));
typedef float f32x4 __attribute__((ext_vector_type(4)));
typedef unsigned u32x2 __attribute__((ext_vector_type(2)));
typedef unsigned u32x4 __attribute__((ext_vector_type(4)));

#define LDS_AS __attribute__((address_space(3)))
#define WAIT_V0() asm volatile("s_waitcnt vmcnt(0)" ::: "memory")

constexpr int S_ = 2048, D_ = 1024, F_ = 3584, H_ = 32, DH_ = 32, NQKV_ = 3072;

__device__ __forceinline__ float bf2f(u16 b) { return __uint_as_float(((uint32_t)b) << 16); }
__device__ __forceinline__ u16 f2bf(float f) {
  __bf16 b = (__bf16)f;
  return __builtin_bit_cast(u16, b);
}
__device__ __forceinline__ float rbf(float f) { return bf2f(f2bf(f)); }
__device__ __forceinline__ float silu_f(float v) { return v / (1.0f + __expf(-v)); }

__device__ __forceinline__ s16x4 tr_read(const char* lds_ptr) {
  return __builtin_amdgcn_ds_read_tr16_b64_v4i16((LDS_AS s16x4*)(lds_ptr));
}

__device__ __forceinline__ void glds16(const void* gsrc, unsigned lds_dst) {
  unsigned keep;
  asm volatile("s_mov_b32 %0, m0\n\ts_mov_b32 m0, %2\n\ts_nop 0\n\tglobal_load_lds_dwordx4 %1, off\n\ts_mov_b32 m0, %0"
               : "=&s"(keep) : "v"(gsrc), "s"(lds_dst) : "memory");
}
__device__ __forceinline__ void glds16_s(const void* sbase, unsigned voff, unsigned lds_dst) {
  unsigned keep;
  asm volatile("s_mov_b32 %0, m0\n\ts_mov_b32 m0, %3\n\ts_nop 0\n\tglobal_load_lds_dwordx4 %1, %2\n\ts_mov_b32 m0, %0"
               : "=&s"(keep) : "v"(voff), "s"(sbase), "s"(lds_dst) : "memory");
}

__global__ __launch_bounds__(256) void ln_kernel(const u16* __restrict__ x, u16* __restrict__ xn, unsigned* __restrict__ cnt) {
  if (blockIdx.x == 0 && threadIdx.x < 64) cnt[threadIdx.x] = 0u;
  const int wid = threadIdx.x >> 6, lane = threadIdx.x & 63;
  const int row = blockIdx.x * 4 + wid;
  const u16* xr = x + (size_t)row * D_;
  float v[16];
  {
    bf16x8 a = *(const bf16x8*)(xr + lane * 8);
    bf16x8 b = *(const bf16x8*)(xr + 512 + lane * 8);
#pragma unroll
    for (int j = 0; j < 8; ++j) { v[j] = bf2f((u16)a[j]); v[8 + j] = bf2f((u16)b[j]); }
  }
  float s = 0.f;
#pragma unroll
  for (int j = 0; j < 16; ++j) s += v[j];
#pragma unroll
  for (int o = 32; o >= 1; o >>= 1) s += __shfl_xor(s, o);
  const float mean = s * (1.0f / D_);
  float q = 0.f;
#pragma unroll
  for (int j = 0; j < 16; ++j) { float d = v[j] - mean; q += d * d; }
#pragma unroll
  for (int o = 32; o >= 1; o >>= 1) q += __shfl_xor(q, o);
  const float var = q * (1.0f / D_);
  const float mb = rbf(mean), vb = rbf(var);
  const float den = rbf(sqrtf(rbf(vb + 1e-5f)));
  bf16x8 a, b;
#pragma unroll
  for (int j = 0; j < 8; ++j) {
    a[j] = (short)f2bf(rbf(v[j] - mb) / den);
    b[j] = (short)f2bf(rbf(v[8 + j] - mb) / den);
  }
  u16* yr = xn + (size_t)row * D_;
  *(bf16x8*)(yr + lane * 8) = a;
  *(bf16x8*)(yr + 512 + lane * 8) = b;
}

constexpr int P1_A_BYTES = 256 * 64 * 2;
constexpr int P1_B_BYTES = 64 * 320 * 2;
constexpr int P1_STAGE = P1_A_BYTES + P1_B_BYTES;

__global__ __launch_bounds__(512, 2) void p1_kernel(const u16* __restrict__ xn, const u16* __restrict__ wqkv,
                                                    const u16* __restrict__ w1, const u16* __restrict__ w3,
                                                    u16* __restrict__ qkv, u16* __restrict__ hbuf) {
  __shared__ __attribute__((aligned(1024))) char smem[2 * P1_STAGE];
  const int tid = threadIdx.x, wid = tid >> 6, lane = tid & 63;
  const int wr = wid >> 2, wc = wid & 3;
  const int bid = blockIdx.x;
  const int ntile = (bid & 7) * 4 + ((bid >> 3) & 3);
  const int mtile = bid >> 5;
  const int m0 = mtile * 256;

  const char* abase = (const char*)(xn + (size_t)m0 * D_);
  const unsigned aofs = (unsigned)((tid >> 3) * D_ * 2 + (((tid & 7) ^ ((tid >> 3) & 7)) * 16));
  const u16* bsrc[5];
  bool bisq[5];
#pragma unroll
  for (int i = 0; i < 5; ++i) {
    const int p = i * 512 + tid, k = p / 40, g = p - k * 40;
    const int sw = ((k >> 1) & 1) | (((k >> 3) & 1) << 1);
    const int c = (g >> 1) ^ sw, n = c * 16 + (g & 1) * 8;
    bisq[i] = n < 96;
    if (n < 96) bsrc[i] = wqkv + (size_t)k * NQKV_ + ntile * 96 + n;
    else if (n < 208) bsrc[i] = w1 + (size_t)k * F_ + ntile * 112 + (n - 96);
    else bsrc[i] = w3 + (size_t)k * F_ + ntile * 112 + (n - 208);
  }

  const int fr = lane & 15, fq = lane >> 4;
  int aoff[2];
#pragma unroll
  for (int ks = 0; ks < 2; ++ks) aoff[ks] = (wr * 128 + fr) * 128 + (((4 * ks + fq) ^ (fr & 7)) * 16);
  int unit[5];
  if (wc < 3) { unit[0] = 6 + 2 * wc; unit[1] = 13 + 2 * wc; unit[2] = 7 + 2 * wc; unit[3] = 14 + 2 * wc; unit[4] = wc; }
  else { unit[0] = 12; unit[1] = 19; unit[2] = 3; unit[3] = 4; unit[4] = 5; }
  int boff[5];
  {
    const int q = fr >> 2, pp = fr & 3;
    const int sw = ((q >> 1) & 1) | ((fq & 1) << 1);
#pragma unroll
    for (int sidx = 0; sidx < 5; ++sidx) boff[sidx] = P1_A_BYTES + 640 * (8 * fq + q) + 8 * pp + 32 * (unit[sidx] ^ sw);
  }

  const int wid_s = __builtin_amdgcn_readfirstlane(wid);
  const unsigned lds0 = (unsigned)(size_t)(LDS_AS char*)smem + wid_s * 1024;
  f32x4 acc[5][8];
#pragma unroll
  for (int s = 0; s < 5; ++s)
#pragma unroll
    for (int m = 0; m < 8; ++m) acc[s][m] = (f32x4){0.f, 0.f, 0.f, 0.f};

  auto stage = [&](int buf, int kt) {
    const unsigned base = lds0 + buf * P1_STAGE;
#pragma unroll
    for (int i = 0; i < 4; ++i) glds16_s(abase + (i * 64 * D_ * 2 + kt * 128), aofs, base + i * 8192);
    const int koq = kt * 64 * NQKV_, kof = kt * 64 * F_;
#pragma unroll
    for (int i = 0; i < 5; ++i) glds16(bsrc[i] + (bisq[i] ? koq : kof), base + P1_A_BYTES + i * 8192);
  };

  stage(0, 0);
  WAIT_V0();
  __syncthreads();
  constexpr int NT = D_ / 64;
  for (int kt = 0; kt < NT; ++kt) {
    const int cur = kt & 1;
    if (kt + 1 < NT) stage(cur ^ 1, kt + 1);
    const char* sb = smem + cur * P1_STAGE;
#pragma unroll
    for (int ks = 0; ks < 2; ++ks) {
      bf16x8 af[8], bfr[5];
#pragma unroll
      for (int m = 0; m < 8; ++m) af[m] = *(const bf16x8*)(sb + aoff[ks] + m * 2048);
#pragma unroll
      for (int s = 0; s < 5; ++s) {
        s16x4 lo = tr_read(sb + boff[s] + ks * 20480);
        s16x4 hi = tr_read(sb + boff[s] + ks * 20480 + 2560);
        bfr[s] = (bf16x8){lo[0], lo[1], lo[2], lo[3], hi[0], hi[1], hi[2], hi[3]};
      }
#pragma unroll
      for (int s = 0; s < 5; ++s)
#pragma unroll
        for (int m = 0; m < 8; ++m)
          acc[s][m] = __builtin_amdgcn_mfma_f32_16x16x32_bf16(bfr[s], af[m], acc[s][m], 0, 0, 0);
    }
    WAIT_V0();
    __syncthreads();
  }

  {
    int elane = __builtin_amdgcn_mbcnt_hi(~0u, __builtin_amdgcn_mbcnt_lo(~0u, 0u));
    asm volatile("" : "+v"(elane));
    const int ewr = wid_s >> 2, ewc = wid_s & 3, efr = elane & 15, efq = elane >> 4;
    const int rowb = m0 + ewr * 128 + efr;
    u16* hp = hbuf + (size_t)rowb * F_ + ntile * 112 + 2 * ewc * 16 + 4 * efq;
    u16* qp = qkv + (size_t)rowb * NQKV_ + ntile * 96 + 4 * efq;
#define P1_STORE_QKV(SL, UNITQ)                                                              \
    _Pragma("unroll") for (int m = 0; m < 8; ++m) {                                          \
      u32x2 w;                                                                               \
      w.x = (unsigned)f2bf(acc[SL][m][0]) | ((unsigned)f2bf(acc[SL][m][1]) << 16);           \
      w.y = (unsigned)f2bf(acc[SL][m][2]) | ((unsigned)f2bf(acc[SL][m][3]) << 16);           \
      *(u32x2*)(qp + (size_t)(16 * m) * NQKV_ + (UNITQ) * 16) = w;                           \
      __builtin_amdgcn_sched_barrier(0);                                                     \
    }
#define P1_STORE_H(SL, COFF)                                                                 \
    _Pragma("unroll") for (int m = 0; m < 8; ++m) {                                          \
      u16 r[4];                                                                              \
      _Pragma("unroll") for (int j = 0; j < 4; ++j) {                                        \
        const float g = rbf(acc[SL][m][j]), u = rbf(acc[SL + 1][m][j]);                      \
        r[j] = f2bf(rbf(silu_f(g)) * u);                                                     \
      }                                                                                      \
      u32x2 w;                                                                               \
      w.x = (unsigned)r[0] | ((unsigned)r[1] << 16);                                         \
      w.y = (unsigned)r[2] | ((unsigned)r[3] << 16);                                         \
      *(u32x2*)(hp + (size_t)(16 * m) * F_ + (COFF)) = w;                                    \
      __builtin_amdgcn_sched_barrier(0);                                                     \
    }
    P1_STORE_H(0, 0)
    if (ewc < 3) { P1_STORE_H(2, 16) P1_STORE_QKV(4, ewc) }
    else { P1_STORE_QKV(2, 3) P1_STORE_QKV(3, 4) P1_STORE_QKV(4, 5) }
  }
}

__global__ __launch_bounds__(256) void attn_kernel(const u16* __restrict__ qkv, u16* __restrict__ o) {
  const int idx = blockIdx.x * 256 + threadIdx.x;
  const int s = idx >> 7, hd = (idx & 127) * 8;
  float q[8];
  {
    bf16x8 t = *(const bf16x8*)(qkv + (size_t)s * NQKV_ + hd);
#pragma unroll
    for (int j = 0; j < 8; ++j) q[j] = bf2f((u16)t[j]);
  }
  bf16x8 kk[8], vv[8];
#pragma unroll
  for (int w = 0; w < 8; ++w) {
    const int r = (s - 2 * w) < 0 ? 0 : (s - 2 * w);
    kk[w] = *(const bf16x8*)(qkv + (size_t)r * NQKV_ + D_ + hd);
    vv[w] = *(const bf16x8*)(qkv + (size_t)r * NQKV_ + 2 * D_ + hd);
  }
  float lg[8];
#pragma unroll
  for (int w = 0; w < 8; ++w) {
    float d = 0.f;
#pragma unroll
    for (int j = 0; j < 8; ++j) d += q[j] * bf2f((u16)kk[w][j]);
    d += __shfl_xor(d, 1);
    d += __shfl_xor(d, 2);
    lg[w] = (s - 2 * w >= 0) ? rbf(rbf(d) * 0.17677669529663687f) : rbf(-1e9f);
  }
  float mx = lg[0];
#pragma unroll
  for (int w = 1; w < 8; ++w) mx = fmaxf(mx, lg[w]);
  float e[8], sum = 0.f;
#pragma unroll
  for (int w = 0; w < 8; ++w) { e[w] = __expf(lg[w] - mx); sum += e[w]; }
  const float inv = 1.0f / sum;
  float acc[8];
#pragma unroll
  for (int j = 0; j < 8; ++j) acc[j] = 0.f;
#pragma unroll
  for (int w = 0; w < 8; ++w) {
    const float p = (s - 2 * w >= 0) ? rbf(e[w] * inv) : 0.f;
#pragma unroll
    for (int j = 0; j < 8; ++j) acc[j] += p * bf2f((u16)vv[w][j]);
  }
  bf16x8 t;
#pragma unroll
  for (int j = 0; j < 8; ++j) t[j] = (short)f2bf(acc[j]);
  *(bf16x8*)(o + (size_t)s * D_ + hd) = t;
}

constexpr int P2_A_BYTES = 256 * 64 * 2;
constexpr int P2_B_BYTES = 64 * 128 * 2;
constexpr int P2_STAGE = P2_A_BYTES + P2_B_BYTES;
constexpr int P2_NSTAGE = 3;

__global__ __launch_bounds__(512, 2) void p2_kernel(const u16* __restrict__ obuf, const u16* __restrict__ hbuf,
                                                    const u16* __restrict__ wo, const u16* __restrict__ w2,
                                                    const u16* __restrict__ x, u16* __restrict__ out,
                                                    float* __restrict__ part, unsigned* __restrict__ cnt) {
  __shared__ __attribute__((aligned(1024))) char smem[P2_NSTAGE * P2_STAGE];
  const int tid = threadIdx.x, wid = tid >> 6, lane = tid & 63;
  const int wr = wid >> 1, wc = wid & 1;
  const int bid = blockIdx.x;
  const int xcd = bid & 7, idx = bid >> 3, split = idx & 3, tt = idx >> 2;
  const int mtile = (xcd >> 1) * 2 + (tt & 1), ntile = (xcd & 1) * 4 + (tt >> 1);
  const int tile = mtile * 8 + ntile;
  const int m0 = mtile * 256, n0 = ntile * 128;

  const unsigned arow = tid >> 3, achunk = ((tid & 7) ^ ((tid >> 3) & 7)) * 16;
  const unsigned aofs_o = arow * (D_ * 2) + achunk, aofs_h = arow * (F_ * 2) + achunk;
  const char* abase_o = (const char*)(obuf + (size_t)m0 * D_);
  const char* abase_h = (const char*)(hbuf + (size_t)m0 * F_);
  const unsigned bkk = tid >> 4;
  const unsigned bsw = (bkk & 3) | (((bkk >> 3) & 1) << 2);
  const unsigned bofs = bkk * (D_ * 2) + ((((tid & 15) >> 1) ^ bsw) * 32) + (tid & 1) * 16;
  const char* bbase_o = (const char*)(wo + n0);
  const char* bbase_h = (const char*)(w2 + n0);

  const int fr = lane & 15, fq = lane >> 4;
  int aoff[2];
#pragma unroll
  for (int ks = 0; ks < 2; ++ks) aoff[ks] = (wr * 64 + fr) * 128 + (((4 * ks + fq) ^ (fr & 7)) * 16);
  int boff[4];
  {
    const int q = fr >> 2, pp = fr & 3;
    const int sw = q | ((fq & 1) << 2);
#pragma unroll
    for (int n = 0; n < 4; ++n) boff[n] = P2_A_BYTES + 256 * (8 * fq + q) + 8 * pp + 32 * ((wc * 4 + n) ^ sw);
  }

  const int wid_s = __builtin_amdgcn_readfirstlane(wid);
  const unsigned lds0 = (unsigned)(size_t)(LDS_AS char*)smem + wid_s * 1024;
  f32x4 accs[4][4], acco[4][4];
#pragma unroll
  for (int n = 0; n < 4; ++n)
#pragma unroll
    for (int m = 0; m < 4; ++m) { accs[n][m] = (f32x4){0.f, 0.f, 0.f, 0.f}; acco[n][m] = (f32x4){0.f, 0.f, 0.f, 0.f}; }

  const int nq = (split == 0) ? 16 : 0;
  const int k2base = (split == 0) ? -16 : 2 + 18 * (split - 1);
  auto stage = [&](int buf, int t) {
    const unsigned base = lds0 + buf * P2_STAGE;
    if (t < nq) {
#pragma unroll
      for (int i = 0; i < 4; ++i) glds16_s(abase_o + (i * 64 * D_ * 2 + t * 128), aofs_o, base + i * 8192);
#pragma unroll
      for (int i = 0; i < 2; ++i) glds16_s(bbase_o + (size_t)(t * 64 + i * 32) * D_ * 2, bofs, base + P2_A_BYTES + i * 8192);
    } else {
      const int k2 = k2base + t;
#pragma unroll
      for (int i = 0; i < 4; ++i) glds16_s(abase_h + (i * 64 * F_ * 2 + k2 * 128), aofs_h, base + i * 8192);
#pragma unroll
      for (int i = 0; i < 2; ++i) glds16_s(bbase_h + (size_t)(k2 * 64 + i * 32) * D_ * 2, bofs, base + P2_A_BYTES + i * 8192);
    }
  };
#define P2_COMPUTE(ACC)                                                                               \
  {                                                                                                   \
    const char* sb = smem + cur * P2_STAGE;                                                           \
    _Pragma("unroll") for (int ks = 0; ks < 2; ++ks) {                                                \
      bf16x8 af[4], bfr[4];                                                                           \
      _Pragma("unroll") for (int m = 0; m < 4; ++m) af[m] = *(const bf16x8*)(sb + aoff[ks] + m * 2048); \
      _Pragma("unroll") for (int n = 0; n < 4; ++n) {                                                 \
        s16x4 lo = tr_read(sb + boff[n] + ks * 8192);                                                 \
        s16x4 hi = tr_read(sb + boff[n] + ks * 8192 + 1024);                                          \
        bfr[n] = (bf16x8){lo[0], lo[1], lo[2], lo[3], hi[0], hi[1], hi[2], hi[3]};                    \
      }                                                                                               \
      _Pragma("unroll") for (int n = 0; n < 4; ++n)                                                   \
        _Pragma("unroll") for (int m = 0; m < 4; ++m)                                                 \
          ACC[n][m] = __builtin_amdgcn_mfma_f32_16x16x32_bf16(bfr[n], af[m], ACC[n][m], 0, 0, 0);     \
    }                                                                                                 \
  }

  constexpr int NT = 18;
  stage(0, 0);
  stage(1, 1);
  int cur = 0, nxt = 2;
  for (int t = 0; t < nq; ++t) {
    asm volatile("s_waitcnt vmcnt(6)" ::: "memory");
    __syncthreads();
    stage(nxt, t + 2);
    P2_COMPUTE(accs)
    cur = (cur == 2) ? 0 : cur + 1;
    nxt = (nxt == 2) ? 0 : nxt + 1;
  }
  for (int t = nq; t < NT - 2; ++t) {
    asm volatile("s_waitcnt vmcnt(6)" ::: "memory");
    __syncthreads();
    stage(nxt, t + 2);
    P2_COMPUTE(acco)
    cur = (cur == 2) ? 0 : cur + 1;
    nxt = (nxt == 2) ? 0 : nxt + 1;
  }
  asm volatile("s_waitcnt vmcnt(6)" ::: "memory");
  __syncthreads();
  P2_COMPUTE(acco)
  cur = (cur == 2) ? 0 : cur + 1;
  asm volatile("s_waitcnt vmcnt(0)" ::: "memory");
  __syncthreads();
  P2_COMPUTE(acco)

  const auto rs = __builtin_amdgcn_make_buffer_rsrc(part, 0, 64u * 20u * 32768u, 0x00020000);
  const unsigned tbase = (unsigned)tile * 20u * 32768u + (unsigned)tid * 16u;
#pragma unroll
  for (int m = 0; m < 4; ++m) {
    if (m != split) {
#pragma unroll
      for (int n = 0; n < 4; ++n)
        __builtin_amdgcn_raw_buffer_store_b128(__builtin_bit_cast(u32x4, acco[n][m]), rs, tbase + (split * 4 + m) * 32768u + n * 8192u, 0, 16);
      if (split == 0) {
#pragma unroll
        for (int n = 0; n < 4; ++n)
          __builtin_amdgcn_raw_buffer_store_b128(__builtin_bit_cast(u32x4, accs[n][m]), rs, tbase + (16 + m) * 32768u + n * 8192u, 0, 16);
      }
    }
  }
  asm volatile("s_waitcnt vmcnt(0)" ::: "memory");
  __syncthreads();
  if (tid == 0) {
    __hip_atomic_fetch_add(cnt + tile, 1u, __ATOMIC_RELAXED, __HIP_MEMORY_SCOPE_AGENT);
    while (__hip_atomic_load(cnt + tile, __ATOMIC_RELAXED, __HIP_MEMORY_SCOPE_AGENT) < 4u) __builtin_amdgcn_s_sleep(1);
  }
  __syncthreads();
  asm volatile("" ::: "memory");
  f32x4 fo[4], fs[4];
#pragma unroll
  for (int n = 0; n < 4; ++n) {
    fo[n] = split == 0 ? acco[n][0] : split == 1 ? acco[n][1] : split == 2 ? acco[n][2] : acco[n][3];
    fs[n] = accs[n][0];
  }
#pragma unroll
  for (int src = 0; src < 4; ++src) {
    if (src != split) {
#pragma unroll
      for (int n = 0; n < 4; ++n)
        fo[n] += __builtin_bit_cast(f32x4, __builtin_amdgcn_raw_buffer_load_b128(rs, tbase + (src * 4 + split) * 32768u + n * 8192u, 0, 16));
    }
  }
  if (split != 0) {
#pragma unroll
    for (int n = 0; n < 4; ++n)
      fs[n] = __builtin_bit_cast(f32x4, __builtin_amdgcn_raw_buffer_load_b128(rs, tbase + (16 + split) * 32768u + n * 8192u, 0, 16));
  }
#pragma unroll
  for (int n = 0; n < 4; ++n) {
    const size_t off = (size_t)(m0 + wr * 64 + 16 * split + fr) * D_ + n0 + (wc * 4 + n) * 16 + 4 * fq;
    const u32x2 xv = *(const u32x2*)(x + off);
    u16 r[4];
#pragma unroll
    for (int j = 0; j < 4; ++j) {
      const unsigned xw = (j < 2) ? xv.x : xv.y;
      const float xf = bf2f((u16)((j & 1) ? (xw >> 16) : (xw & 0xffff)));
      const float sv = rbf(fs[n][j]);
      const float g = rbf(silu_f(rbf(fo[n][j])));
      r[j] = f2bf(xf + rbf(sv * g));
    }
    u32x2 w;
    w.x = (unsigned)r[0] | ((unsigned)r[1] << 16);
    w.y = (unsigned)r[2] | ((unsigned)r[3] << 16);
    *(u32x2*)(out + off) = w;
  }
}

extern "C" void kernel_launch(void* const* d_in, const int* in_sizes, int n_in, void* d_out, int out_size, void* d_ws,
                              size_t ws_size, hipStream_t stream) {
  const u16* x = (const u16*)d_in[0];
  const u16* wqkv = (const u16*)d_in[1];
  const u16* wo = (const u16*)d_in[2];
  const u16* w1 = (const u16*)d_in[4];
  const u16* w3 = (const u16*)d_in[5];
  const u16* w2 = (const u16*)d_in[6];
  u16* out = (u16*)d_out;
  char* ws = (char*)d_ws;
  u16* xn = (u16*)ws;
  u16* qkv = (u16*)(ws + (size_t)S_ * D_ * 2);
  u16* hbuf = qkv + (size_t)S_ * NQKV_;
  u16* obuf = hbuf + (size_t)S_ * F_;
  float* part = (float*)(obuf + (size_t)S_ * D_);
  unsigned* cnt = (unsigned*)((char*)part + (size_t)64 * 20 * 32768);

  ln_kernel<<<S_ / 4, 256, 0, stream>>>(x, xn, cnt);
  p1_kernel<<<256, 512, 0, stream>>>(xn, wqkv, w1, w3, qkv, hbuf);
  attn_kernel<<<S_ * H_ * 4 / 256, 256, 0, stream>>>(qkv, obuf);
  p2_kernel<<<256, 512, 0, stream>>>(obuf, hbuf, wo, w2, x, out, part, cnt);
}
```
